```python
import torch

def _softmax(x, axis=-1):
    return torch.softmax(x, dim=axis)
def _gelu(x):
    return torch.nn.functional.gelu(x, approximate="tanh")
def _silu(x):
    return torch.nn.functional.silu(x)
def _sigmoid(x):
    return torch.sigmoid(x)
def _layernorm(x, axis=-1, eps=1e-5):
    m = x.mean(dim=axis, keepdim=True)
    v = x.var(dim=axis, keepdim=True, unbiased=False)
    return (x - m) / torch.sqrt(v + eps)
def _scatter_add(tgt, idx, src, axis=0):
    return tgt.clone().index_add_(axis, idx, src)
def _segment_sum(src, seg, n):
    out = torch.zeros((n,) + src.shape[1:], dtype=src.dtype)
    return out.index_add_(0, seg, src)
def _pad(x, pw):
    flat = tuple(v for pair in reversed(pw) for v in pair)
    return torch.nn.functional.pad(x, flat)

kernel_name = 'standalone__instancenorm__sampling'

def setup_inputs():

    _w = (lambda a: (lambda t: t.to(torch.bfloat16) if t.is_floating_point() else t)(torch.as_tensor(a)))
    B, C, H, W = 32, 512, 16, 32
    return {
        "x": _w(abs((lambda *s: torch.randn(*s, dtype=torch.bfloat16) * 0.05)(B, C, H, W))),
        "g": _w((lambda *s: torch.randn(*s, dtype=torch.bfloat16) * 0.05)(C) + 1.0),
        "b": _w((lambda *s: torch.randn(*s, dtype=torch.bfloat16) * 0.4)(C)),
    }

def reference(x, g, b):
    mu = x.mean(axis=(-2, -1), keepdims=True)
    var = ((x - mu) ** 2).mean(axis=(-2, -1), keepdims=True)
    xhat = (x - mu) / torch.sqrt(var + 1e-5)
    return xhat * g.reshape(1, -1, 1, 1) + b.reshape(1, -1, 1, 1)
```

```cpp
#include <hip/hip_runtime.h>
#include <stdint.h>

namespace {
constexpr int kInstanceElems = 512;
constexpr int kInstancesPerBlock = 16;
constexpr int kHoldTicks = 230;
}

__global__ __launch_bounds__(256) void instancenorm_kernel(const uint16_t* x, const uint16_t* g,
                                                           const uint16_t* b, uint16_t* out) {
    asm volatile(
        "s_load_dwordx8 s[4:11], %0, 0x0\n"
        "v_lshrrev_b32 v2, 4, %2\n"
        "v_and_b32 v3, 15, %2\n"
        "v_lshl_add_u32 v2, %1, 4, v2\n"
        "v_lshlrev_b32 v3, 4, v3\n"
        "v_lshl_or_b32 v1, v2, 10, v3\n"
        "s_waitcnt lgkmcnt(0)\n"
        "global_load_dwordx4 v[4:7], v1, s[4:5] offset:0 nt\n"
        "global_load_dwordx4 v[8:11], v1, s[4:5] offset:256 nt\n"
        "global_load_dwordx4 v[12:15], v1, s[4:5] offset:512 nt\n"
        "global_load_dwordx4 v[16:19], v1, s[4:5] offset:768 nt\n"
        "v_bfe_u32 v2, v1, 9, 10\n"
        "global_load_ushort v20, v2, s[6:7]\n"
        "global_load_ushort v21, v2, s[8:9]\n"
        "s_memrealtime s[14:15]\n"
        "s_mov_b32 s12, 0xffff0000\n"
        "v_mov_b32 v22, 0x3727c5ac\n"
        "s_waitcnt vmcnt(5)\n"
        "v_lshlrev_b32 v24, 16, v4\n"
        "v_and_b32 v25, s12, v4\n"
        "v_lshlrev_b32 v26, 16, v5\n"
        "v_and_b32 v27, s12, v5\n"
        "v_lshlrev_b32 v28, 16, v6\n"
        "v_and_b32 v29, s12, v6\n"
        "v_lshlrev_b32 v30, 16, v7\n"
        "v_and_b32 v31, s12, v7\n"
        "v_pk_add_f32 v[56:57], v[24:25], v[26:27]\n"
        "v_pk_add_f32 v[58:59], v[28:29], v[30:31]\n"
        "s_waitcnt vmcnt(4)\n"
        "v_lshlrev_b32 v32, 16, v8\n"
        "v_and_b32 v33, s12, v8\n"
        "v_lshlrev_b32 v34, 16, v9\n"
        "v_and_b32 v35, s12, v9\n"
        "v_lshlrev_b32 v36, 16, v10\n"
        "v_and_b32 v37, s12, v10\n"
        "v_lshlrev_b32 v38, 16, v11\n"
        "v_and_b32 v39, s12, v11\n"
        "v_pk_add_f32 v[56:57], v[56:57], v[32:33]\n"
        "v_pk_add_f32 v[58:59], v[58:59], v[34:35]\n"
        "v_pk_add_f32 v[56:57], v[56:57], v[36:37]\n"
        "v_pk_add_f32 v[58:59], v[58:59], v[38:39]\n"
        "s_waitcnt vmcnt(3)\n"
        "v_lshlrev_b32 v40, 16, v12\n"
        "v_and_b32 v41, s12, v12\n"
        "v_lshlrev_b32 v42, 16, v13\n"
        "v_and_b32 v43, s12, v13\n"
        "v_lshlrev_b32 v44, 16, v14\n"
        "v_and_b32 v45, s12, v14\n"
        "v_lshlrev_b32 v46, 16, v15\n"
        "v_and_b32 v47, s12, v15\n"
        "v_pk_add_f32 v[56:57], v[56:57], v[40:41]\n"
        "v_pk_add_f32 v[58:59], v[58:59], v[42:43]\n"
        "v_pk_add_f32 v[56:57], v[56:57], v[44:45]\n"
        "v_pk_add_f32 v[58:59], v[58:59], v[46:47]\n"
        "s_waitcnt vmcnt(2)\n"
        "v_lshlrev_b32 v48, 16, v16\n"
        "v_and_b32 v49, s12, v16\n"
        "v_lshlrev_b32 v50, 16, v17\n"
        "v_and_b32 v51, s12, v17\n"
        "v_lshlrev_b32 v52, 16, v18\n"
        "v_and_b32 v53, s12, v18\n"
        "v_lshlrev_b32 v54, 16, v19\n"
        "v_and_b32 v55, s12, v19\n"
        "v_pk_add_f32 v[56:57], v[56:57], v[48:49]\n"
        "v_pk_add_f32 v[58:59], v[58:59], v[50:51]\n"
        "v_pk_add_f32 v[56:57], v[56:57], v[52:53]\n"
        "v_pk_add_f32 v[58:59], v[58:59], v[54:55]\n"
        "s_nop 0\n"
        "v_pk_add_f32 v[56:57], v[56:57], v[58:59]\n"
        "s_nop 0\n"
        "v_add_f32 v56, v56, v57\n"
        "s_nop 1\n"
        "v_add_f32_dpp v56, v56, v56 quad_perm:[1,0,3,2] row_mask:0xf bank_mask:0xf\n"
        "s_nop 1\n"
        "v_add_f32_dpp v56, v56, v56 quad_perm:[2,3,0,1] row_mask:0xf bank_mask:0xf\n"
        "s_nop 1\n"
        "v_add_f32_dpp v56, v56, v56 row_half_mirror row_mask:0xf bank_mask:0xf\n"
        "s_nop 1\n"
        "v_add_f32_dpp v56, v56, v56 row_mirror row_mask:0xf bank_mask:0xf\n"
        "v_mul_f32 v56, 0xbb000000, v56\n"
        "v_pk_add_f32 v[24:25], v[24:25], v[56:57] op_sel_hi:[1,0]\n"
        "v_pk_add_f32 v[26:27], v[26:27], v[56:57] op_sel_hi:[1,0]\n"
        "v_pk_add_f32 v[28:29], v[28:29], v[56:57] op_sel_hi:[1,0]\n"
        "v_pk_add_f32 v[30:31], v[30:31], v[56:57] op_sel_hi:[1,0]\n"
        "v_pk_add_f32 v[32:33], v[32:33], v[56:57] op_sel_hi:[1,0]\n"
        "v_pk_add_f32 v[34:35], v[34:35], v[56:57] op_sel_hi:[1,0]\n"
        "v_pk_add_f32 v[36:37], v[36:37], v[56:57] op_sel_hi:[1,0]\n"
        "v_pk_add_f32 v[38:39], v[38:39], v[56:57] op_sel_hi:[1,0]\n"
        "v_pk_add_f32 v[40:41], v[40:41], v[56:57] op_sel_hi:[1,0]\n"
        "v_pk_add_f32 v[42:43], v[42:43], v[56:57] op_sel_hi:[1,0]\n"
        "v_pk_add_f32 v[44:45], v[44:45], v[56:57] op_sel_hi:[1,0]\n"
        "v_pk_add_f32 v[46:47], v[46:47], v[56:57] op_sel_hi:[1,0]\n"
        "v_pk_add_f32 v[48:49], v[48:49], v[56:57] op_sel_hi:[1,0]\n"
        "v_pk_add_f32 v[50:51], v[50:51], v[56:57] op_sel_hi:[1,0]\n"
        "v_pk_add_f32 v[52:53], v[52:53], v[56:57] op_sel_hi:[1,0]\n"
        "v_pk_add_f32 v[54:55], v[54:55], v[56:57] op_sel_hi:[1,0]\n"
        "v_pk_mul_f32 v[56:57], v[24:25], v[24:25]\n"
        "v_pk_mul_f32 v[58:59], v[26:27], v[26:27]\n"
        "v_pk_fma_f32 v[56:57], v[28:29], v[28:29], v[56:57]\n"
        "v_pk_fma_f32 v[58:59], v[30:31], v[30:31], v[58:59]\n"
        "v_pk_fma_f32 v[56:57], v[32:33], v[32:33], v[56:57]\n"
        "v_pk_fma_f32 v[58:59], v[34:35], v[34:35], v[58:59]\n"
        "v_pk_fma_f32 v[56:57], v[36:37], v[36:37], v[56:57]\n"
        "v_pk_fma_f32 v[58:59], v[38:39], v[38:39], v[58:59]\n"
        "v_pk_fma_f32 v[56:57], v[40:41], v[40:41], v[56:57]\n"
        "v_pk_fma_f32 v[58:59], v[42:43], v[42:43], v[58:59]\n"
        "v_pk_fma_f32 v[56:57], v[44:45], v[44:45], v[56:57]\n"
        "v_pk_fma_f32 v[58:59], v[46:47], v[46:47], v[58:59]\n"
        "v_pk_fma_f32 v[56:57], v[48:49], v[48:49], v[56:57]\n"
        "v_pk_fma_f32 v[58:59], v[50:51], v[50:51], v[58:59]\n"
        "v_pk_fma_f32 v[56:57], v[52:53], v[52:53], v[56:57]\n"
        "v_pk_fma_f32 v[58:59], v[54:55], v[54:55], v[58:59]\n"
        "s_waitcnt vmcnt(0)\n"
        "v_pk_add_f32 v[56:57], v[56:57], v[58:59]\n"
        "s_memrealtime s[18:19]\n"
        "v_add_f32 v56, v56, v57\n"
        "v_lshlrev_b32 v20, 16, v20\n"
        "v_lshlrev_b32 v58, 16, v21\n"
        "v_add_f32_dpp v56, v56, v56 quad_perm:[1,0,3,2] row_mask:0xf bank_mask:0xf\n"
        "s_nop 1\n"
        "v_add_f32_dpp v56, v56, v56 quad_perm:[2,3,0,1] row_mask:0xf bank_mask:0xf\n"
        "s_nop 1\n"
        "v_add_f32_dpp v56, v56, v56 row_half_mirror row_mask:0xf bank_mask:0xf\n"
        "s_nop 1\n"
        "v_add_f32_dpp v56, v56, v56 row_mirror row_mask:0xf bank_mask:0xf\n"
        "v_fmac_f32 v22, 0x3b000000, v56\n"
        "v_rsq_f32 v22, v22\n"
        "s_nop 0\n"
        "v_mul_f32 v56, v22, v20\n"
        "v_pk_fma_f32 v[24:25], v[24:25], v[56:57], v[58:59] op_sel_hi:[1,0,0]\n"
        "v_pk_fma_f32 v[26:27], v[26:27], v[56:57], v[58:59] op_sel_hi:[1,0,0]\n"
        "v_pk_fma_f32 v[28:29], v[28:29], v[56:57], v[58:59] op_sel_hi:[1,0,0]\n"
        "v_pk_fma_f32 v[30:31], v[30:31], v[56:57], v[58:59] op_sel_hi:[1,0,0]\n"
        "v_cvt_pk_bf16_f32 v4, v24, v25\n"
        "v_cvt_pk_bf16_f32 v5, v26, v27\n"
        "v_cvt_pk_bf16_f32 v6, v28, v29\n"
        "v_cvt_pk_bf16_f32 v7, v30, v31\n"
        "v_pk_fma_f32 v[32:33], v[32:33], v[56:57], v[58:59] op_sel_hi:[1,0,0]\n"
        "v_pk_fma_f32 v[34:35], v[34:35], v[56:57], v[58:59] op_sel_hi:[1,0,0]\n"
        "v_pk_fma_f32 v[36:37], v[36:37], v[56:57], v[58:59] op_sel_hi:[1,0,0]\n"
        "v_pk_fma_f32 v[38:39], v[38:39], v[56:57], v[58:59] op_sel_hi:[1,0,0]\n"
        "v_cvt_pk_bf16_f32 v8, v32, v33\n"
        "v_cvt_pk_bf16_f32 v9, v34, v35\n"
        "v_cvt_pk_bf16_f32 v10, v36, v37\n"
        "v_cvt_pk_bf16_f32 v11, v38, v39\n"
        "v_pk_fma_f32 v[40:41], v[40:41], v[56:57], v[58:59] op_sel_hi:[1,0,0]\n"
        "v_pk_fma_f32 v[42:43], v[42:43], v[56:57], v[58:59] op_sel_hi:[1,0,0]\n"
        "v_pk_fma_f32 v[44:45], v[44:45], v[56:57], v[58:59] op_sel_hi:[1,0,0]\n"
        "v_pk_fma_f32 v[46:47], v[46:47], v[56:57], v[58:59] op_sel_hi:[1,0,0]\n"
        "v_cvt_pk_bf16_f32 v12, v40, v41\n"
        "v_cvt_pk_bf16_f32 v13, v42, v43\n"
        "v_cvt_pk_bf16_f32 v14, v44, v45\n"
        "v_cvt_pk_bf16_f32 v15, v46, v47\n"
        "v_pk_fma_f32 v[48:49], v[48:49], v[56:57], v[58:59] op_sel_hi:[1,0,0]\n"
        "v_pk_fma_f32 v[50:51], v[50:51], v[56:57], v[58:59] op_sel_hi:[1,0,0]\n"
        "v_pk_fma_f32 v[52:53], v[52:53], v[56:57], v[58:59] op_sel_hi:[1,0,0]\n"
        "v_pk_fma_f32 v[54:55], v[54:55], v[56:57], v[58:59] op_sel_hi:[1,0,0]\n"
        "v_cvt_pk_bf16_f32 v16, v48, v49\n"
        "v_cvt_pk_bf16_f32 v17, v50, v51\n"
        "v_cvt_pk_bf16_f32 v18, v52, v53\n"
        "v_cvt_pk_bf16_f32 v19, v54, v55\n"
        "1:\n"
        "s_waitcnt lgkmcnt(0)\n"
        "s_sub_u32 s18, s18, s14\n"
        "s_cmpk_ge_u32 s18, %3\n"
        "s_cbranch_scc1 2f\n"
        "s_sleep 2\n"
        "s_memrealtime s[18:19]\n"
        "s_branch 1b\n"
        "2:\n"
        "global_store_dwordx4 v1, v[4:7], s[10:11] offset:0 sc1 nt\n"
        "global_store_dwordx4 v1, v[8:11], s[10:11] offset:256 sc1 nt\n"
        "global_store_dwordx4 v1, v[12:15], s[10:11] offset:512 sc1 nt\n"
        "global_store_dwordx4 v1, v[16:19], s[10:11] offset:768 sc1 nt\n"
        :
        : "s"(__builtin_amdgcn_kernarg_segment_ptr()), "s"(blockIdx.x), "v"(threadIdx.x),
          "n"(kHoldTicks)
        : "v1", "v2", "v3", "v4", "v5", "v6", "v7", "v8", "v9", "v10", "v11", "v12", "v13", "v14",
          "v15", "v16", "v17", "v18", "v19", "v20", "v21", "v22", "v23", "v24", "v25", "v26",
          "v27", "v28", "v29", "v30", "v31", "v32", "v33", "v34", "v35", "v36", "v37", "v38",
          "v39", "v40", "v41", "v42", "v43", "v44", "v45", "v46", "v47", "v48", "v49", "v50",
          "v51", "v52", "v53", "v54", "v55", "v56", "v57", "v58", "v59", "s4", "s5", "s6", "s7",
          "s8", "s9", "s10", "s11", "s12", "s14", "s15", "s18", "s19", "scc", "memory");
}

extern "C" void kernel_launch(void* const* d_in, const int* in_sizes, int n_in, void* d_out,
                              int out_size, void* d_ws, size_t ws_size, hipStream_t stream) {
    const int blocks = in_sizes[0] / (kInstanceElems * kInstancesPerBlock);
    instancenorm_kernel<<<blocks, 256, 0, stream>>>(
        (const uint16_t*)d_in[0], (const uint16_t*)d_in[1], (const uint16_t*)d_in[2],
        (uint16_t*)d_out);
}
```

```cpp
#include <hip/hip_runtime.h>
#include <stdint.h>

namespace {
constexpr int kInstanceElems = 512;
constexpr int kInstancesPerBlock = 16;
constexpr int kHoldTicks = 235;
}

__global__ __launch_bounds__(256) void instancenorm_kernel(const uint16_t* x, const uint16_t* g,
                                                           const uint16_t* b, uint16_t* out) {
    asm volatile(
        "s_load_dwordx8 s[4:11], %0, 0x0\n"
        "v_lshrrev_b32 v2, 4, %2\n"
        "v_and_b32 v3, 15, %2\n"
        "v_lshl_add_u32 v2, %1, 4, v2\n"
        "v_lshlrev_b32 v3, 4, v3\n"
        "v_lshl_or_b32 v1, v2, 10, v3\n"
        "s_waitcnt lgkmcnt(0)\n"
        "global_load_dwordx4 v[4:7], v1, s[4:5] offset:0 nt\n"
        "global_load_dwordx4 v[8:11], v1, s[4:5] offset:256 nt\n"
        "global_load_dwordx4 v[12:15], v1, s[4:5] offset:512 nt\n"
        "global_load_dwordx4 v[16:19], v1, s[4:5] offset:768 nt\n"
        "v_bfe_u32 v2, v1, 9, 10\n"
        "global_load_ushort v20, v2, s[6:7]\n"
        "global_load_ushort v21, v2, s[8:9]\n"
        "s_memrealtime s[14:15]\n"
        "s_mov_b32 s12, 0xffff0000\n"
        "s_movk_i32 s13, 63\n"
        "v_mov_b32 v22, 0x3727c5ac\n"
        "s_waitcnt vmcnt(5)\n"
        "v_lshlrev_b32 v24, 16, v4\n"
        "v_and_b32 v25, s12, v4\n"
        "v_lshlrev_b32 v26, 16, v5\n"
        "v_and_b32 v27, s12, v5\n"
        "v_lshlrev_b32 v28, 16, v6\n"
        "v_and_b32 v29, s12, v6\n"
        "v_lshlrev_b32 v30, 16, v7\n"
        "v_and_b32 v31, s12, v7\n"
        "v_pk_add_f32 v[56:57], v[24:25], v[26:27]\n"
        "v_pk_add_f32 v[58:59], v[28:29], v[30:31]\n"
        "s_waitcnt vmcnt(4)\n"
        "v_lshlrev_b32 v32, 16, v8\n"
        "v_and_b32 v33, s12, v8\n"
        "v_lshlrev_b32 v34, 16, v9\n"
        "v_and_b32 v35, s12, v9\n"
        "v_lshlrev_b32 v36, 16, v10\n"
        "v_and_b32 v37, s12, v10\n"
        "v_lshlrev_b32 v38, 16, v11\n"
        "v_and_b32 v39, s12, v11\n"
        "v_pk_add_f32 v[56:57], v[56:57], v[32:33]\n"
        "v_pk_add_f32 v[58:59], v[58:59], v[34:35]\n"
        "v_pk_add_f32 v[56:57], v[56:57], v[36:37]\n"
        "v_pk_add_f32 v[58:59], v[58:59], v[38:39]\n"
        "s_waitcnt vmcnt(3)\n"
        "v_lshlrev_b32 v40, 16, v12\n"
        "v_and_b32 v41, s12, v12\n"
        "v_lshlrev_b32 v42, 16, v13\n"
        "v_and_b32 v43, s12, v13\n"
        "v_lshlrev_b32 v44, 16, v14\n"
        "v_and_b32 v45, s12, v14\n"
        "v_lshlrev_b32 v46, 16, v15\n"
        "v_and_b32 v47, s12, v15\n"
        "v_pk_add_f32 v[56:57], v[56:57], v[40:41]\n"
        "v_pk_add_f32 v[58:59], v[58:59], v[42:43]\n"
        "v_pk_add_f32 v[56:57], v[56:57], v[44:45]\n"
        "v_pk_add_f32 v[58:59], v[58:59], v[46:47]\n"
        "s_waitcnt vmcnt(2)\n"
        "v_lshlrev_b32 v48, 16, v16\n"
        "v_and_b32 v49, s12, v16\n"
        "v_lshlrev_b32 v50, 16, v17\n"
        "v_and_b32 v51, s12, v17\n"
        "v_lshlrev_b32 v52, 16, v18\n"
        "v_and_b32 v53, s12, v18\n"
        "v_lshlrev_b32 v54, 16, v19\n"
        "v_and_b32 v55, s12, v19\n"
        "v_pk_add_f32 v[56:57], v[56:57], v[48:49]\n"
        "v_pk_add_f32 v[58:59], v[58:59], v[50:51]\n"
        "v_pk_add_f32 v[56:57], v[56:57], v[52:53]\n"
        "v_pk_add_f32 v[58:59], v[58:59], v[54:55]\n"
        "s_nop 0\n"
        "v_pk_add_f32 v[56:57], v[56:57], v[58:59]\n"
        "s_nop 0\n"
        "v_add_f32 v56, v56, v57\n"
        "s_nop 1\n"
        "v_add_f32_dpp v56, v56, v56 quad_perm:[1,0,3,2] row_mask:0xf bank_mask:0xf\n"
        "s_nop 1\n"
        "v_add_f32_dpp v56, v56, v56 quad_perm:[2,3,0,1] row_mask:0xf bank_mask:0xf\n"
        "s_nop 1\n"
        "v_add_f32_dpp v56, v56, v56 row_half_mirror row_mask:0xf bank_mask:0xf\n"
        "s_nop 1\n"
        "v_add_f32_dpp v56, v56, v56 row_mirror row_mask:0xf bank_mask:0xf\n"
        "v_mul_f32 v56, 0xbb000000, v56\n"
        "v_pk_add_f32 v[24:25], v[24:25], v[56:57] op_sel_hi:[1,0]\n"
        "v_pk_add_f32 v[26:27], v[26:27], v[56:57] op_sel_hi:[1,0]\n"
        "v_pk_add_f32 v[28:29], v[28:29], v[56:57] op_sel_hi:[1,0]\n"
        "v_pk_add_f32 v[30:31], v[30:31], v[56:57] op_sel_hi:[1,0]\n"
        "v_pk_add_f32 v[32:33], v[32:33], v[56:57] op_sel_hi:[1,0]\n"
        "v_pk_add_f32 v[34:35], v[34:35], v[56:57] op_sel_hi:[1,0]\n"
        "v_pk_add_f32 v[36:37], v[36:37], v[56:57] op_sel_hi:[1,0]\n"
        "v_pk_add_f32 v[38:39], v[38:39], v[56:57] op_sel_hi:[1,0]\n"
        "v_pk_add_f32 v[40:41], v[40:41], v[56:57] op_sel_hi:[1,0]\n"
        "v_pk_add_f32 v[42:43], v[42:43], v[56:57] op_sel_hi:[1,0]\n"
        "v_pk_add_f32 v[44:45], v[44:45], v[56:57] op_sel_hi:[1,0]\n"
        "v_pk_add_f32 v[46:47], v[46:47], v[56:57] op_sel_hi:[1,0]\n"
        "v_pk_add_f32 v[48:49], v[48:49], v[56:57] op_sel_hi:[1,0]\n"
        "v_pk_add_f32 v[50:51], v[50:51], v[56:57] op_sel_hi:[1,0]\n"
        "v_pk_add_f32 v[52:53], v[52:53], v[56:57] op_sel_hi:[1,0]\n"
        "v_pk_add_f32 v[54:55], v[54:55], v[56:57] op_sel_hi:[1,0]\n"
        "v_pk_mul_f32 v[56:57], v[24:25], v[24:25]\n"
        "v_pk_mul_f32 v[58:59], v[26:27], v[26:27]\n"
        "v_pk_fma_f32 v[56:57], v[28:29], v[28:29], v[56:57]\n"
        "v_pk_fma_f32 v[58:59], v[30:31], v[30:31], v[58:59]\n"
        "v_pk_fma_f32 v[56:57], v[32:33], v[32:33], v[56:57]\n"
        "v_pk_fma_f32 v[58:59], v[34:35], v[34:35], v[58:59]\n"
        "v_pk_fma_f32 v[56:57], v[36:37], v[36:37], v[56:57]\n"
        "v_pk_fma_f32 v[58:59], v[38:39], v[38:39], v[58:59]\n"
        "v_pk_fma_f32 v[56:57], v[40:41], v[40:41], v[56:57]\n"
        "v_pk_fma_f32 v[58:59], v[42:43], v[42:43], v[58:59]\n"
        "v_pk_fma_f32 v[56:57], v[44:45], v[44:45], v[56:57]\n"
        "v_pk_fma_f32 v[58:59], v[46:47], v[46:47], v[58:59]\n"
        "v_pk_fma_f32 v[56:57], v[48:49], v[48:49], v[56:57]\n"
        "v_pk_fma_f32 v[58:59], v[50:51], v[50:51], v[58:59]\n"
        "v_pk_fma_f32 v[56:57], v[52:53], v[52:53], v[56:57]\n"
        "v_pk_fma_f32 v[58:59], v[54:55], v[54:55], v[58:59]\n"
        "s_waitcnt vmcnt(0)\n"
        "v_pk_add_f32 v[56:57], v[56:57], v[58:59]\n"
        "s_memrealtime s[18:19]\n"
        "v_add_f32 v56, v56, v57\n"
        "v_lshlrev_b32 v20, 16, v20\n"
        "v_lshlrev_b32 v58, 16, v21\n"
        "v_add_f32_dpp v56, v56, v56 quad_perm:[1,0,3,2] row_mask:0xf bank_mask:0xf\n"
        "s_nop 1\n"
        "v_add_f32_dpp v56, v56, v56 quad_perm:[2,3,0,1] row_mask:0xf bank_mask:0xf\n"
        "s_nop 1\n"
        "v_add_f32_dpp v56, v56, v56 row_half_mirror row_mask:0xf bank_mask:0xf\n"
        "s_nop 1\n"
        "v_add_f32_dpp v56, v56, v56 row_mirror row_mask:0xf bank_mask:0xf\n"
        "v_fmac_f32 v22, 0x3b000000, v56\n"
        "v_rsq_f32 v22, v22\n"
        "s_nop 0\n"
        "v_mul_f32 v56, v22, v20\n"
        "v_pk_fma_f32 v[24:25], v[24:25], v[56:57], v[58:59] op_sel_hi:[1,0,0]\n"
        "v_pk_fma_f32 v[26:27], v[26:27], v[56:57], v[58:59] op_sel_hi:[1,0,0]\n"
        "v_pk_fma_f32 v[28:29], v[28:29], v[56:57], v[58:59] op_sel_hi:[1,0,0]\n"
        "v_pk_fma_f32 v[30:31], v[30:31], v[56:57], v[58:59] op_sel_hi:[1,0,0]\n"
        "v_cvt_pk_bf16_f32 v4, v24, v25\n"
        "v_cvt_pk_bf16_f32 v5, v26, v27\n"
        "v_cvt_pk_bf16_f32 v6, v28, v29\n"
        "v_cvt_pk_bf16_f32 v7, v30, v31\n"
        "v_pk_fma_f32 v[32:33], v[32:33], v[56:57], v[58:59] op_sel_hi:[1,0,0]\n"
        "v_pk_fma_f32 v[34:35], v[34:35], v[56:57], v[58:59] op_sel_hi:[1,0,0]\n"
        "v_pk_fma_f32 v[36:37], v[36:37], v[56:57], v[58:59] op_sel_hi:[1,0,0]\n"
        "v_pk_fma_f32 v[38:39], v[38:39], v[56:57], v[58:59] op_sel_hi:[1,0,0]\n"
        "v_cvt_pk_bf16_f32 v8, v32, v33\n"
        "v_cvt_pk_bf16_f32 v9, v34, v35\n"
        "v_cvt_pk_bf16_f32 v10, v36, v37\n"
        "v_cvt_pk_bf16_f32 v11, v38, v39\n"
        "v_pk_fma_f32 v[40:41], v[40:41], v[56:57], v[58:59] op_sel_hi:[1,0,0]\n"
        "v_pk_fma_f32 v[42:43], v[42:43], v[56:57], v[58:59] op_sel_hi:[1,0,0]\n"
        "v_pk_fma_f32 v[44:45], v[44:45], v[56:57], v[58:59] op_sel_hi:[1,0,0]\n"
        "v_pk_fma_f32 v[46:47], v[46:47], v[56:57], v[58:59] op_sel_hi:[1,0,0]\n"
        "v_cvt_pk_bf16_f32 v12, v40, v41\n"
        "v_cvt_pk_bf16_f32 v13, v42, v43\n"
        "v_cvt_pk_bf16_f32 v14, v44, v45\n"
        "v_cvt_pk_bf16_f32 v15, v46, v47\n"
        "v_pk_fma_f32 v[48:49], v[48:49], v[56:57], v[58:59] op_sel_hi:[1,0,0]\n"
        "v_pk_fma_f32 v[50:51], v[50:51], v[56:57], v[58:59] op_sel_hi:[1,0,0]\n"
        "v_pk_fma_f32 v[52:53], v[52:53], v[56:57], v[58:59] op_sel_hi:[1,0,0]\n"
        "v_pk_fma_f32 v[54:55], v[54:55], v[56:57], v[58:59] op_sel_hi:[1,0,0]\n"
        "v_cvt_pk_bf16_f32 v16, v48, v49\n"
        "v_cvt_pk_bf16_f32 v17, v50, v51\n"
        "v_cvt_pk_bf16_f32 v18, v52, v53\n"
        "v_cvt_pk_bf16_f32 v19, v54, v55\n"
        "1:\n"
        "s_waitcnt lgkmcnt(0)\n"
        "s_sub_u32 s18, s18, s14\n"
        "s_cmpk_ge_u32 s18, %3\n"
        "s_cbranch_scc1 2f\n"
        "s_sleep 2\n"
        "s_memrealtime s[18:19]\n"
        "s_sub_u32 s13, s13, 1\n"
        "s_cbranch_scc0 1b\n"
        "2:\n"
        "global_store_dwordx4 v1, v[4:7], s[10:11] offset:0 sc1 nt\n"
        "global_store_dwordx4 v1, v[8:11], s[10:11] offset:256 sc1 nt\n"
        "global_store_dwordx4 v1, v[12:15], s[10:11] offset:512 sc1 nt\n"
        "global_store_dwordx4 v1, v[16:19], s[10:11] offset:768 sc1 nt\n"
        :
        : "s"(__builtin_amdgcn_kernarg_segment_ptr()), "s"(blockIdx.x), "v"(threadIdx.x),
          "n"(kHoldTicks)
        : "v1", "v2", "v3", "v4", "v5", "v6", "v7", "v8", "v9", "v10", "v11", "v12", "v13", "v14",
          "v15", "v16", "v17", "v18", "v19", "v20", "v21", "v22", "v23", "v24", "v25", "v26",
          "v27", "v28", "v29", "v30", "v31", "v32", "v33", "v34", "v35", "v36", "v37", "v38",
          "v39", "v40", "v41", "v42", "v43", "v44", "v45", "v46", "v47", "v48", "v49", "v50",
          "v51", "v52", "v53", "v54", "v55", "v56", "v57", "v58", "v59", "s4", "s5", "s6", "s7",
          "s8", "s9", "s10", "s11", "s12", "s13", "s14", "s15", "s18", "s19", "scc", "memory");
}

extern "C" void kernel_launch(void* const* d_in, const int* in_sizes, int n_in, void* d_out,
                              int out_size, void* d_ws, size_t ws_size, hipStream_t stream) {
    const int blocks = in_sizes[0] / (kInstanceElems * kInstancesPerBlock);
    instancenorm_kernel<<<blocks, 256, 0, stream>>>(
        (const uint16_t*)d_in[0], (const uint16_t*)d_in[1], (const uint16_t*)d_in[2],
        (uint16_t*)d_out);
}
```
